```python
import torch

def _softmax(x, axis=-1):
    return torch.softmax(x, dim=axis)
def _gelu(x):
    return torch.nn.functional.gelu(x, approximate="tanh")
def _silu(x):
    return torch.nn.functional.silu(x)
def _sigmoid(x):
    return torch.sigmoid(x)
def _layernorm(x, axis=-1, eps=1e-5):
    m = x.mean(dim=axis, keepdim=True)
    v = x.var(dim=axis, keepdim=True, unbiased=False)
    return (x - m) / torch.sqrt(v + eps)
def _scatter_add(tgt, idx, src, axis=0):
    return tgt.clone().index_add_(axis, idx, src)
def _segment_sum(src, seg, n):
    out = torch.zeros((n,) + src.shape[1:], dtype=src.dtype)
    return out.index_add_(0, seg, src)
def _pad(x, pw):
    flat = tuple(v for pair in reversed(pw) for v in pair)
    return torch.nn.functional.pad(x, flat)

kernel_name = 'recurrent_scan__spectral_gfnet__dense_activation_silu__prompt'

def setup_inputs():

    _w = (lambda a: (lambda t: t.to(torch.bfloat16) if t.is_floating_point() else t)(torch.as_tensor(a)))
    def _A_setup():
        B, S, D = 1, 2048, 512
        Df = D // 2 + 1
        return {
            "x": _w((lambda *s: torch.randn(*s, dtype=torch.bfloat16) * 0.05)(B, S, D)),
            "w_r": _w((lambda *s: torch.randn(*s, dtype=torch.bfloat16) * 3.2)(S, Df) * 0.02),
            "w_i": _w((lambda *s: torch.randn(*s, dtype=torch.bfloat16) * 3.2)(S, Df) * 0.02),
        }
    _A_in = _A_setup()

    def _B_setup():
        B, S, D, F = 1, 2048, 512, 1376
        return {
            "x": _w((lambda *s: torch.randn(*s, dtype=torch.bfloat16) * 0.05)(B, S, D)),
            "w1": _w((lambda *s: torch.randn(*s, dtype=torch.bfloat16) * 3.2)(D, F) * (D ** -0.5)),
            "w2": _w((lambda *s: torch.randn(*s, dtype=torch.bfloat16) * 0.4)(F, D) * (F ** -0.5)),
        }
    _B_in = _B_setup()

    return {
        "A_x": _A_in["x"],
        "A_w_r": _A_in["w_r"],
        "A_w_i": _A_in["w_i"],
        "B_x": _B_in["x"],
        "B_w1": _B_in["w1"],
        "B_w2": _B_in["w2"],
    }

def reference(A_x, A_w_r, A_w_i, B_x, B_w1, B_w2):
    def _A_ref(x, w_r, w_i):
        B, S, D = x.shape
        X = (lambda x, axes=(-2,-1): torch.fft.rfft2(x.float(), dim=axes))(x, axes=(-2, -1))
        W = w_r + 1j * w_i
        X = X * W
        y = (lambda x, s, axes=(-2,-1): torch.fft.irfft2(x, s=s, dim=axes).to(torch.bfloat16))(X, (S, D), axes=(-2, -1))
        return y

    def _B_ref(x, w1, w2):
        h = torch.einsum("bsd,df->bsf", x, w1)
        h = _silu(h)
        out = torch.einsum("bsf,fd->bsd", h, w2)
        return out

    x = A_x
    xn = _layernorm(x, axis=-1)
    s = _A_ref(xn, A_w_r, A_w_i)
    g = _silu(_B_ref(xn, B_w1, B_w2))
    return x + s * g
```

```cpp
#include <hip/hip_runtime.h>
#include <hip/hip_bf16.h>
#include <stdint.h>

#define S_ 2048
#define D_ 512
#define F_ 1376
#define DF_ 257

typedef unsigned short u16;
typedef unsigned int u32;
typedef __attribute__((ext_vector_type(8))) short bf16x8;
typedef __attribute__((ext_vector_type(4))) short s16x4;
typedef __attribute__((ext_vector_type(4))) float f32x4;
typedef __attribute__((ext_vector_type(4))) unsigned int u32x4;
typedef __attribute__((ext_vector_type(2))) unsigned int u32x2;
typedef __attribute__((ext_vector_type(2))) float f32x2v;
typedef __attribute__((ext_vector_type(2))) __bf16 bf16x2v;

#define DEVI __device__ __forceinline__

DEVI float bf2f(u16 v) { return __uint_as_float(((u32)v) << 16); }
DEVI u16 f2bf(float f) {
  f32x2v x = {f, 0.f};
  bf16x2v r = __builtin_convertvector(x, bf16x2v);
  return (u16)(__builtin_bit_cast(u32, r) & 0xffffu);
}
DEVI u32 pack2bf(float a, float b) {
  f32x2v x = {a, b};
  bf16x2v r = __builtin_convertvector(x, bf16x2v);
  return __builtin_bit_cast(u32, r);
}
DEVI float rbf(float f) { return bf2f(f2bf(f)); }
DEVI float silu_f(float x) { return x / (1.0f + expf(-x)); }

DEVI float2 cmul(float2 a, float2 b) { return make_float2(a.x * b.x - a.y * b.y, a.x * b.y + a.y * b.x); }
DEVI float2 cadd(float2 a, float2 b) { return make_float2(a.x + b.x, a.y + b.y); }
DEVI float2 csub(float2 a, float2 b) { return make_float2(a.x - b.x, a.y - b.y); }
template <int SGN> DEVI float2 mul_mi(float2 a) { return SGN < 0 ? make_float2(a.y, -a.x) : make_float2(-a.y, a.x); }

template <int SGN> DEVI void fft4(float2& a0, float2& a1, float2& a2, float2& a3) {
  float2 c0 = cadd(a0, a2), d0 = csub(a0, a2);
  float2 c1 = cadd(a1, a3), d1 = mul_mi<SGN>(csub(a1, a3));
  a0 = cadd(c0, c1); a2 = csub(c0, c1);
  a1 = cadd(d0, d1); a3 = csub(d0, d1);
}
template <int SGN> DEVI void fft8(float2* v) {
  const float R = 0.70710678118654752440f;
  float2 a0 = cadd(v[0], v[4]), b0 = csub(v[0], v[4]);
  float2 a1 = cadd(v[1], v[5]), b1 = csub(v[1], v[5]);
  float2 a2 = cadd(v[2], v[6]), b2 = csub(v[2], v[6]);
  float2 a3 = cadd(v[3], v[7]), b3 = csub(v[3], v[7]);
  if (SGN < 0) {
    b1 = make_float2((b1.x + b1.y) * R, (b1.y - b1.x) * R);
    b3 = make_float2((b3.y - b3.x) * R, -(b3.x + b3.y) * R);
  } else {
    b1 = make_float2((b1.x - b1.y) * R, (b1.y + b1.x) * R);
    b3 = make_float2(-(b3.x + b3.y) * R, (b3.x - b3.y) * R);
  }
  b2 = mul_mi<SGN>(b2);
  fft4<SGN>(a0, a1, a2, a3);
  fft4<SGN>(b0, b1, b2, b3);
  v[0] = a0; v[2] = a1; v[4] = a2; v[6] = a3;
  v[1] = b0; v[3] = b1; v[5] = b2; v[7] = b3;
}

DEVI int phys(int i) { return i + (i >> 3); }

template <int SGN> DEVI void wave_fft512(float2* v, float2* buf  , const float2* tw  , int lane) {
  fft8<SGN>(v);
#pragma unroll
  for (int r = 0; r < 8; ++r) buf[phys(lane * 8 + r)] = v[r];
  __syncthreads();
#pragma unroll
  for (int r = 0; r < 8; ++r) {
    float2 x = buf[phys(lane + 64 * r)];
    float2 w = tw[(8 * (lane & 7) * r) & 511];
    if (SGN > 0) w.y = -w.y;
    v[r] = cmul(x, w);
  }
  fft8<SGN>(v);
  __syncthreads();
#pragma unroll
  for (int r = 0; r < 8; ++r) buf[phys((lane >> 3) * 64 + (lane & 7) + 8 * r)] = v[r];
  __syncthreads();
#pragma unroll
  for (int r = 0; r < 8; ++r) {
    float2 x = buf[phys(lane + 64 * r)];
    float2 w = tw[(lane * r) & 511];
    if (SGN > 0) w.y = -w.y;
    v[r] = cmul(x, w);
  }
  fft8<SGN>(v);
}

template <int BM, int BN, bool LN, int KTOT, int NTOT, int EPI>
DEVI void gemm_tile(const u16* __restrict__ A, int lda, const u16* __restrict__ B, int ldb, u16* __restrict__ C, int ldc,
                    int m0, int n0, char* lds) {
  constexpr int BK = 64;
  constexpr int SB = BN * 2 + 32;
  constexpr int ABYTES = BM * BK * 2;
  constexpr int BBYTES = BK * SB;
  constexpr int WM = BM / 2, WN = BN / 2;
  constexpr int MI = WM / 16, NI = WN / 16;
  constexpr int ACH = BM * 8 / 256;
  constexpr int BCH = BK * (BN / 8) / 256;
  constexpr int NKT = (KTOT + BK - 1) / BK;
  const int tid = threadIdx.x, lane = tid & 63, wave = tid >> 6;
  const int wm0 = (wave >> 1) * WM, wn0 = (wave & 1) * WN;
  float* stat = (float*)(lds + 2 * (ABYTES + BBYTES));

  if (LN) {
    for (int rr = 0; rr < BM / 4; ++rr) {
      const int row = wave * (BM / 4) + rr;
      u32x4 q = *(const u32x4*)(A + (size_t)(m0 + row) * lda + lane * 8);
      float f[8];
#pragma unroll
      for (int j = 0; j < 4; ++j) { f[2 * j] = __uint_as_float(q[j] << 16); f[2 * j + 1] = __uint_as_float(q[j] & 0xffff0000u); }
      float s = 0.f;
#pragma unroll
      for (int j = 0; j < 8; ++j) s += f[j];
#pragma unroll
      for (int o = 32; o > 0; o >>= 1) s += __shfl_xor(s, o);
      const float mean = s * (1.0f / D_);
      float s2 = 0.f;
#pragma unroll
      for (int j = 0; j < 8; ++j) { float d = f[j] - mean; s2 += d * d; }
#pragma unroll
      for (int o = 32; o > 0; o >>= 1) s2 += __shfl_xor(s2, o);
      if (lane == 0) {
        stat[row * 2] = rbf(mean);
        stat[row * 2 + 1] = rbf(sqrtf(rbf(rbf(s2 * (1.0f / D_)) + 1e-5f)));
      }
    }
    __syncthreads();
  }

  u32x4 ra[ACH], rb[BCH];
  auto gload = [&](int kt) {
#pragma unroll
    for (int c = 0; c < ACH; ++c) {
      const int ch = tid + c * 256, row = ch >> 3, kc = ch & 7;
      const int k = kt * BK + kc * 8;
      u32x4 q = {0u, 0u, 0u, 0u};
      if ((KTOT % BK == 0) || k < KTOT) q = *(const u32x4*)(A + (size_t)(m0 + row) * lda + k);
      ra[c] = q;
    }
#pragma unroll
    for (int c = 0; c < BCH; ++c) {
      const int ch = tid + c * 256, krow = ch / (BN / 8), nc = ch % (BN / 8);
      const int k = kt * BK + krow, n = n0 + nc * 8;
      u32x4 q = {0u, 0u, 0u, 0u};
      if (((KTOT % BK == 0) || k < KTOT) && ((NTOT % BN == 0) || n < NTOT)) q = *(const u32x4*)(B + (size_t)k * ldb + n);
      rb[c] = q;
    }
  };
  auto lstore = [&](int buf) {
    char* la = lds + buf * (ABYTES + BBYTES);
    char* lb = la + ABYTES;
#pragma unroll
    for (int c = 0; c < ACH; ++c) {
      const int ch = tid + c * 256, row = ch >> 3, kc = ch & 7;
      u32x4 q = ra[c];
      if (LN) {
        const float mu = stat[row * 2], sd = stat[row * 2 + 1];
#pragma unroll
        for (int j = 0; j < 4; ++j) {
          float lo = __uint_as_float(q[j] << 16), hi = __uint_as_float(q[j] & 0xffff0000u);
          lo = rbf(lo - mu) / sd; hi = rbf(hi - mu) / sd;
          q[j] = pack2bf(lo, hi);
        }
      }
      *(u32x4*)(la + row * 128 + ((kc ^ (row & 7)) << 4)) = q;
    }
#pragma unroll
    for (int c = 0; c < BCH; ++c) {
      const int ch = tid + c * 256, krow = ch / (BN / 8), nc = ch % (BN / 8);
      *(u32x4*)(lb + krow * SB + nc * 16) = rb[c];
    }
  };

  f32x4 acc[MI][NI];
#pragma unroll
  for (int i = 0; i < MI; ++i)
#pragma unroll
    for (int j = 0; j < NI; ++j) acc[i][j] = (f32x4){0.f, 0.f, 0.f, 0.f};

  gload(0);
  lstore(0);
  __syncthreads();
  const int g = lane >> 4, q4 = (lane & 15) >> 2, p4 = lane & 3;
  for (int kt = 0; kt < NKT; ++kt) {
    const int cur = kt & 1;
    if (kt + 1 < NKT) gload(kt + 1);
    const char* la = lds + cur * (ABYTES + BBYTES);
    const char* lb = la + ABYTES;
#pragma unroll
    for (int ks = 0; ks < 2; ++ks) {
      bf16x8 af[MI], bfr[NI];
#pragma unroll
      for (int i = 0; i < MI; ++i) {
        const int row = wm0 + i * 16 + (lane & 15);
        af[i] = *(const bf16x8*)(la + row * 128 + (((ks * 4 + g) ^ (row & 7)) << 4));
      }
#pragma unroll
      for (int j = 0; j < NI; ++j) {
        const int krow = ks * 32 + 8 * g + q4;
        const int col = wn0 + j * 16 + 4 * p4;
        s16x4 t0 = __builtin_amdgcn_ds_read_tr16_b64_v4i16((__attribute__((address_space(3))) s16x4*)(lb + krow * SB + col * 2));
        s16x4 t1 = __builtin_amdgcn_ds_read_tr16_b64_v4i16((__attribute__((address_space(3))) s16x4*)(lb + (krow + 4) * SB + col * 2));
        bfr[j] = (bf16x8){t0[0], t0[1], t0[2], t0[3], t1[0], t1[1], t1[2], t1[3]};
      }
#pragma unroll
      for (int i = 0; i < MI; ++i)
#pragma unroll
        for (int j = 0; j < NI; ++j)
          acc[i][j] = __builtin_amdgcn_mfma_f32_16x16x32_bf16(bfr[j], af[i], acc[i][j], 0, 0, 0);
    }
    if (kt + 1 < NKT) lstore(cur ^ 1);
    __syncthreads();
  }
#pragma unroll
  for (int i = 0; i < MI; ++i) {
    const int m = m0 + wm0 + i * 16 + (lane & 15);
#pragma unroll
    for (int j = 0; j < NI; ++j) {
      const int n = n0 + wn0 + j * 16 + 4 * g;
      if ((NTOT % BN == 0) || n < NTOT) {
        float o[4];
#pragma unroll
        for (int r = 0; r < 4; ++r) o[r] = silu_f(rbf(acc[i][j][r]));
        u32x2 w = {pack2bf(o[0], o[1]), pack2bf(o[2], o[3])};
        *(u32x2*)(C + (size_t)m * ldc + n) = w;
      }
    }
  }
}

#define K1_NA 256
#define G1_BM 128
#define G1_BN 128
#define G1_NTN ((F_ + G1_BN - 1) / G1_BN)
#define K1_NG ((S_ / G1_BM) * G1_NTN)

__global__ __launch_bounds__(256) void k1_kernel(const u16* __restrict__ x, const u16* __restrict__ wr, const u16* __restrict__ wi,
                                                 const u16* __restrict__ w1, float2* __restrict__ X1T, u32* __restrict__ WT,
                                                 u16* __restrict__ h) {
  __shared__ __attribute__((aligned(16))) char lds[47168];
  const int tid = threadIdx.x, lane = tid & 63, wave = tid >> 6;
  float2* tw = (float2*)lds;
  float2* buf = tw + 512 + wave * 576;
  float2* sX = tw + 512 + 4 * 576;
  u32* sW = (u32*)(sX + 256 * 8);
  const int s0 = blockIdx.x * 8;
  const int ra_ = s0 + 2 * wave, rb_ = ra_ + 1;
  u32x4 qa = *(const u32x4*)(x + (size_t)ra_ * D_ + lane * 8);
  u32x4 qb = *(const u32x4*)(x + (size_t)rb_ * D_ + lane * 8);
  for (int i = tid; i < 8 * DF_; i += 256) {
    const int r = i / DF_, k = i % DF_;
    sW[k * 8 + r] = (u32)wr[(size_t)(s0 + r) * DF_ + k] | ((u32)wi[(size_t)(s0 + r) * DF_ + k] << 16);
  }
  for (int i = tid; i < 512; i += 256) {
    float s, c;
    sincospif(-(float)i * (1.0f / 256.0f), &s, &c);
    tw[i] = make_float2(c, s);
  }
  float fa[8], fb[8];
#pragma unroll
  for (int j = 0; j < 4; ++j) {
    fa[2 * j] = __uint_as_float(qa[j] << 16); fa[2 * j + 1] = __uint_as_float(qa[j] & 0xffff0000u);
    fb[2 * j] = __uint_as_float(qb[j] << 16); fb[2 * j + 1] = __uint_as_float(qb[j] & 0xffff0000u);
  }
  float sa = 0.f, sb = 0.f;
#pragma unroll
  for (int j = 0; j < 8; ++j) { sa += fa[j]; sb += fb[j]; }
#pragma unroll
  for (int o = 32; o > 0; o >>= 1) { sa += __shfl_xor(sa, o); sb += __shfl_xor(sb, o); }
  const float ma = sa * (1.0f / D_), mb = sb * (1.0f / D_);
  float va = 0.f, vb = 0.f;
#pragma unroll
  for (int j = 0; j < 8; ++j) { float d = fa[j] - ma; va += d * d; d = fb[j] - mb; vb += d * d; }
#pragma unroll
  for (int o = 32; o > 0; o >>= 1) { va += __shfl_xor(va, o); vb += __shfl_xor(vb, o); }
  const float mua = rbf(ma), mub = rbf(mb);
  const float sda = rbf(sqrtf(rbf(rbf(va * (1.0f / D_)) + 1e-5f)));
  const float sdb = rbf(sqrtf(rbf(rbf(vb * (1.0f / D_)) + 1e-5f)));
#pragma unroll
  for (int j = 0; j < 8; ++j) {
    float2 z = make_float2(rbf(rbf(fa[j] - mua) / sda), rbf(rbf(fb[j] - mub) / sdb));
    buf[phys(lane * 8 + j)] = z;
    fa[j] = z.x; fb[j] = z.y;
  }
  {
    u32x4 oa, ob;
#pragma unroll
    for (int j = 0; j < 4; ++j) { oa[j] = pack2bf(fa[2 * j], fa[2 * j + 1]); ob[j] = pack2bf(fb[2 * j], fb[2 * j + 1]); }
    *(u32x4*)(h + (size_t)ra_ * D_ + lane * 8) = oa;
    *(u32x4*)(h + (size_t)rb_ * D_ + lane * 8) = ob;
  }
  __syncthreads();
  float2 v[8];
#pragma unroll
  for (int r = 0; r < 8; ++r) v[r] = buf[phys(lane + 64 * r)];
  __syncthreads();
  wave_fft512<-1>(v, buf, tw, lane);
  __syncthreads();
#pragma unroll
  for (int r = 0; r < 8; ++r) buf[phys(lane + 64 * r)] = v[r];
  __syncthreads();
#pragma unroll
  for (int r = 0; r < 4; ++r) {
    const int k = lane + 64 * r;
    const float2 zk = v[r];
    const float2 zm = buf[phys((512 - k) & 511)];
    float2 xa = make_float2(0.5f * (zk.x + zm.x), 0.5f * (zk.y - zm.y));
    float2 xb = make_float2(0.5f * (zk.y + zm.y), -0.5f * (zk.x - zm.x));
    if (k == 0) {
      const float2 zn = buf[phys(256)];
      xa = make_float2(zk.x, zn.x);
      xb = make_float2(zk.y, zn.y);
    }
    sX[k * 8 + 2 * wave] = xa;
    sX[k * 8 + 2 * wave + 1] = xb;
  }
  __syncthreads();
  {
    const int k0 = tid >> 2, part = tid & 3;
#pragma unroll
    for (int it = 0; it < 4; ++it) {
      const int k = k0 + 64 * it;
      f32x4 val = *(const f32x4*)(sX + k * 8 + part * 2);
      *(f32x4*)(X1T + (size_t)k * S_ + s0 + part * 2) = val;
    }
    for (int i = tid; i < DF_ * 2; i += 256) {
      const int k = i >> 1, part2 = i & 1;
      u32x4 val = *(const u32x4*)(sW + k * 8 + part2 * 4);
      *(u32x4*)(WT + (size_t)k * S_ + s0 + part2 * 4) = val;
    }
  }
}

#define K2_NC 256
#define G2_BM 64
#define G2_BN 64
#define K2_NG ((S_ / G2_BM) * (D_ / G2_BN))

template <int SGN> DEVI void col_fft2048(float2* v, float2* bufA, float2* bufB, const float2* tw, int t) {
  fft8<SGN>(v);
#pragma unroll
  for (int r = 0; r < 8; ++r) bufA[phys(t * 8 + r)] = v[r];
  __syncthreads();
#pragma unroll
  for (int r = 0; r < 8; ++r) {
    float2 xx = bufA[phys(t + 256 * r)];
    float2 w = tw[(32 * (t & 7) * r) & 2047];
    if (SGN > 0) w.y = -w.y;
    v[r] = cmul(xx, w);
  }
  fft8<SGN>(v);
#pragma unroll
  for (int r = 0; r < 8; ++r) bufB[phys((t >> 3) * 64 + (t & 7) + 8 * r)] = v[r];
  __syncthreads();
#pragma unroll
  for (int r = 0; r < 8; ++r) {
    float2 xx = bufB[phys(t + 256 * r)];
    float2 w = tw[(4 * (t & 63) * r) & 2047];
    if (SGN > 0) w.y = -w.y;
    v[r] = cmul(xx, w);
  }
  fft8<SGN>(v);
#pragma unroll
  for (int r = 0; r < 8; ++r) bufA[phys((t >> 6) * 512 + (t & 63) + 64 * r)] = v[r];
  __syncthreads();
  float2 u[8];
#pragma unroll
  for (int hh = 0; hh < 2; ++hh) {
    const int j = t + 256 * hh;
#pragma unroll
    for (int r = 0; r < 4; ++r) {
      float2 xx = bufA[phys(j + 512 * r)];
      float2 w = tw[(j * r) & 2047];
      if (SGN > 0) w.y = -w.y;
      u[hh * 4 + r] = cmul(xx, w);
    }
    fft4<SGN>(u[hh * 4], u[hh * 4 + 1], u[hh * 4 + 2], u[hh * 4 + 3]);
  }
#pragma unroll
  for (int hh = 0; hh < 2; ++hh)
#pragma unroll
    for (int r = 0; r < 4; ++r) v[hh + 2 * r] = u[hh * 4 + r];
}

__global__ __launch_bounds__(256) void k2_kernel(const float2* __restrict__ X1T, const u32* __restrict__ WT, float2* __restrict__ Y1T,
                                                 const u16* __restrict__ h, const u16* __restrict__ w2, u16* __restrict__ gout) {
  __shared__ __attribute__((aligned(16))) char lds[53248];
  const int t = threadIdx.x;
  float2* tw = (float2*)lds;
  float2* bufA = tw + 2048;
  float2* bufB = bufA + 2304;
  const int col = blockIdx.x;
  float2 v[8];
#pragma unroll
  for (int r = 0; r < 8; ++r) v[r] = X1T[(size_t)col * S_ + t + 256 * r];
  u32 wv[8];
#pragma unroll
  for (int r = 0; r < 8; ++r) wv[r] = WT[(size_t)col * S_ + t + 256 * r];
  u32 wm[8], wn[8], wnm[8];
  if (col == 0) {
#pragma unroll
    for (int r = 0; r < 8; ++r) {
      const int i = t + 256 * r;
      wm[r] = WT[(S_ - i) & (S_ - 1)];
      wn[r] = WT[(size_t)256 * S_ + i];
      wnm[r] = WT[(size_t)256 * S_ + ((S_ - i) & (S_ - 1))];
    }
  }
  for (int i = t; i < 2048; i += 256) {
    float s, c;
    sincospif(-(float)i * (1.0f / 1024.0f), &s, &c);
    tw[i] = make_float2(c, s);
  }
  __syncthreads();
  col_fft2048<-1>(v, bufA, bufB, tw, t);
  if (col == 0) {
    __syncthreads();
#pragma unroll
    for (int r = 0; r < 8; ++r) bufB[phys(t + 256 * r)] = v[r];
    __syncthreads();
#pragma unroll
    for (int r = 0; r < 8; ++r) {
      const int i = t + 256 * r;
      const float2 zk = v[r], zm = bufB[phys((S_ - i) & (S_ - 1))];
      const float2 a = make_float2(0.5f * (zk.x + zm.x), 0.5f * (zk.y - zm.y));
      const float2 bb = make_float2(0.5f * (zk.y + zm.y), -0.5f * (zk.x - zm.x));
      const float2 w0 = make_float2(0.5f * (bf2f(wv[r] & 0xffff) + bf2f(wm[r] & 0xffff)), 0.5f * (bf2f(wv[r] >> 16) - bf2f(wm[r] >> 16)));
      const float2 w1_ = make_float2(0.5f * (bf2f(wn[r] & 0xffff) + bf2f(wnm[r] & 0xffff)), 0.5f * (bf2f(wn[r] >> 16) - bf2f(wnm[r] >> 16)));
      const float2 u = cmul(a, w0), vv = cmul(bb, w1_);
      v[r] = make_float2(u.x - vv.y, u.y + vv.x);
    }
    __syncthreads();
  } else {
#pragma unroll
    for (int r = 0; r < 8; ++r) v[r] = cmul(v[r], make_float2(bf2f(wv[r] & 0xffff), bf2f(wv[r] >> 16)));
    __syncthreads();
  }
  col_fft2048<1>(v, bufA, bufB, tw, t);
#pragma unroll
  for (int r = 0; r < 8; ++r) Y1T[(size_t)col * S_ + t + 256 * r] = v[r];
}

__global__ __launch_bounds__(256) void g1_kernel(const u16* __restrict__ xn, const u16* __restrict__ w1, u16* __restrict__ h) {
  __shared__ __attribute__((aligned(16))) char lds[36864];
  const int mt = blockIdx.x / 22, nt = blockIdx.x % 22;
  gemm_tile<64, 64, false, D_, F_, 0>(xn, D_, w1, F_, h, F_, mt * 64, nt * 64, lds);
}
__global__ __launch_bounds__(256) void g2_kernel(const u16* __restrict__ h, const u16* __restrict__ w2, u16* __restrict__ g) {
  __shared__ __attribute__((aligned(16))) char lds[36864];
  const int mt = blockIdx.x / 8, nt = blockIdx.x % 8;
  gemm_tile<64, 64, false, F_, D_, 1>(h, F_, w2, D_, g, D_, mt * 64, nt * 64, lds);
}
__global__ __launch_bounds__(256) void k3_kernel(const float2* __restrict__ Y1T, const u16* __restrict__ x, const u16* __restrict__ g,
                                                 u16* __restrict__ out) {
  __shared__ __attribute__((aligned(16))) char lds[(512 + 4 * 576 + 256 * 8) * 8];
  const int tid = threadIdx.x, lane = tid & 63, wave = tid >> 6;
  float2* tw = (float2*)lds;
  float2* buf = tw + 512 + wave * 576;
  float2* sY = tw + 512 + 4 * 576;
  const int s0 = blockIdx.x * 8;
  {
    const int k0 = tid >> 2, part = tid & 3;
#pragma unroll
    for (int it = 0; it < 4; ++it) {
      const int k = k0 + 64 * it;
      *(f32x4*)(sY + k * 8 + part * 2) = *(const f32x4*)(Y1T + (size_t)k * S_ + s0 + part * 2);
    }
  }
  for (int i = tid; i < 512; i += 256) {
    float s, c;
    sincospif(-(float)i * (1.0f / 256.0f), &s, &c);
    tw[i] = make_float2(c, s);
  }
  const int ra_ = s0 + 2 * wave, rb_ = ra_ + 1;
  u16 xa[8], xb[8], ga[8], gb[8];
#pragma unroll
  for (int r = 0; r < 8; ++r) {
    const int n = lane + 64 * r;
    xa[r] = x[(size_t)ra_ * D_ + n]; xb[r] = x[(size_t)rb_ * D_ + n];
    ga[r] = g[(size_t)ra_ * D_ + n]; gb[r] = g[(size_t)rb_ * D_ + n];
  }
  __syncthreads();
  float2 v[8];
#pragma unroll
  for (int r = 0; r < 8; ++r) {
    const int n = lane + 64 * r;
    float2 z;
    if (n == 0) {
      const float2 ya = sY[2 * wave], yb = sY[2 * wave + 1];
      z = make_float2(ya.x, yb.x);
    } else if (n == 256) {
      const float2 ya = sY[2 * wave], yb = sY[2 * wave + 1];
      z = make_float2(ya.y, yb.y);
    } else if (n < 256) {
      const float2 ya = sY[n * 8 + 2 * wave], yb = sY[n * 8 + 2 * wave + 1];
      z = make_float2(ya.x - yb.y, ya.y + yb.x);
    } else {
      const float2 ya = sY[(512 - n) * 8 + 2 * wave], yb = sY[(512 - n) * 8 + 2 * wave + 1];
      z = make_float2(ya.x + yb.y, yb.x - ya.y);
    }
    v[r] = z;
  }
  wave_fft512<1>(v, buf, tw, lane);
  const float sc = 1.0f / ((float)S_ * (float)D_);
#pragma unroll
  for (int r = 0; r < 8; ++r) {
    const int n = lane + 64 * r;
    const float sa = rbf(v[r].x * sc), sb = rbf(v[r].y * sc);
    out[(size_t)ra_ * D_ + n] = f2bf(bf2f(xa[r]) + rbf(sa * bf2f(ga[r])));
    out[(size_t)rb_ * D_ + n] = f2bf(bf2f(xb[r]) + rbf(sb * bf2f(gb[r])));
  }
}

extern "C" void kernel_launch(void* const* d_in, const int* in_sizes, int n_in, void* d_out, int out_size, void* d_ws,
                              size_t ws_size, hipStream_t stream) {
  const u16* x = (const u16*)d_in[0];
  const u16* wr = (const u16*)d_in[1];
  const u16* wi = (const u16*)d_in[2];
  const u16* w1 = (const u16*)d_in[4];
  const u16* w2 = (const u16*)d_in[5];
  char* ws = (char*)d_ws;
  float2* X1T = (float2*)ws;
  float2* Y1T = (float2*)(ws + (4u << 20));
  u32* WT = (u32*)(ws + (8u << 20));
  u16* h = (u16*)(ws + (12u << 20));
  u16* g = (u16*)(ws + (20u << 20));
  u16* xn = (u16*)(ws + (24u << 20));
  k1_kernel<<<K1_NA, 256, 0, stream>>>(x, wr, wi, w1, X1T, WT, xn);
  g1_kernel<<<32 * 22, 256, 0, stream>>>(xn, w1, h);
  k2_kernel<<<K2_NC, 256, 0, stream>>>(X1T, WT, Y1T, h, w2, g);
  g2_kernel<<<256, 256, 0, stream>>>(h, w2, g);
  k3_kernel<<<S_ / 8, 256, 0, stream>>>(Y1T, x, g, (u16*)d_out);
}
```
